```python
import torch

def _softmax(x, axis=-1):
    return torch.softmax(x, dim=axis)
def _gelu(x):
    return torch.nn.functional.gelu(x, approximate="tanh")
def _silu(x):
    return torch.nn.functional.silu(x)
def _sigmoid(x):
    return torch.sigmoid(x)
def _layernorm(x, axis=-1, eps=1e-5):
    m = x.mean(dim=axis, keepdim=True)
    v = x.var(dim=axis, keepdim=True, unbiased=False)
    return (x - m) / torch.sqrt(v + eps)
def _scatter_add(tgt, idx, src, axis=0):
    return tgt.clone().index_add_(axis, idx, src)
def _segment_sum(src, seg, n):
    out = torch.zeros((n,) + src.shape[1:], dtype=src.dtype)
    return out.index_add_(0, seg, src)
def _pad(x, pw):
    flat = tuple(v for pair in reversed(pw) for v in pair)
    return torch.nn.functional.pad(x, flat)

kernel_name = 'standalone__qk_norm__prompt'

def setup_inputs():

    _w = (lambda a: (lambda t: t.to(torch.bfloat16) if t.is_floating_point() else t)(torch.as_tensor(a)))
    B, H, S, D = 2, 32, 2048, 640
    Dh = D // H
    return {
        "q": _w((lambda *s: torch.randn(*s, dtype=torch.bfloat16) * 0.05)(B, H, S, Dh)),
        "k": _w((lambda *s: torch.randn(*s, dtype=torch.bfloat16) * 0.05)(B, H, S, Dh)),
        "v": _w((lambda *s: torch.randn(*s, dtype=torch.bfloat16) * 0.8)(B, H, S, Dh)),
        "t": _w(torch.abs((lambda *s: torch.randn(*s, dtype=torch.bfloat16) * 0.8)(H)) + 1.0),
    }

def reference(q, k, v, t):
    eps = 1e-6
    qn = q / (torch.sqrt((q * q).sum(axis=-1, keepdims=True)) + eps)
    kn = k / (torch.sqrt((k * k).sum(axis=-1, keepdims=True)) + eps)
    logits = torch.einsum('bhqd,bhkd->bhqk', qn, kn) * t[None, :, None, None]
    attn = _softmax(logits, axis=-1)
    return torch.einsum('bhqk,bhkd->bhqd', attn, v)
```

```cpp
#include <hip/hip_runtime.h>
#include <hip/hip_bf16.h>
typedef float f32x16 __attribute__((ext_vector_type(16)));
typedef float f32x32 __attribute__((ext_vector_type(32)));
typedef float f32x8 __attribute__((ext_vector_type(8)));
typedef float f32x2 __attribute__((ext_vector_type(2)));
typedef unsigned u32x4 __attribute__((ext_vector_type(4)));
typedef unsigned u32x2 __attribute__((ext_vector_type(2)));
typedef unsigned u32x4a __attribute__((ext_vector_type(4), aligned(4)));
typedef unsigned u32x2a __attribute__((ext_vector_type(2), aligned(4)));
typedef __bf16 bf16x2 __attribute__((ext_vector_type(2)));
typedef __bf16 bf16x8 __attribute__((ext_vector_type(8)));
typedef __bf16 bf16x16 __attribute__((ext_vector_type(16)));
typedef short s16x4 __attribute__((ext_vector_type(4)));
typedef short s16x8 __attribute__((ext_vector_type(8)));
#define DI __device__ __forceinline__
#define LDSP(T, off) ((__attribute__((address_space(3))) T*)(size_t)(off))

constexpr int SEQ = 2048, DH = 20;
constexpr int BLK = 5120;
constexpr int LDS_BYTES = 32 * BLK;
constexpr float LOG2E = 1.4426950408889634f;
constexpr unsigned T_SAFE_BITS = 0x41300000u;

struct Row { u32x4 a, b; u32x2 c; };

DI float blo(unsigned x) { return __uint_as_float(x << 16); }
DI float bhi(unsigned x) { return __uint_as_float(x & 0xffff0000u); }
DI unsigned pk(float lo, float hi) { f32x2 v = {lo, hi}; return __builtin_bit_cast(unsigned, __builtin_convertvector(v, bf16x2)); }
DI float dot2(unsigned a, unsigned b, float c) {
  return __builtin_amdgcn_fdot2_f32_bf16(__builtin_bit_cast(bf16x2, a), __builtin_bit_cast(bf16x2, b), c, false);
}
DI float inv_norm(const Row& r) {
  float s0 = dot2(r.c[0], r.c[0], 0.f), s1 = dot2(r.c[1], r.c[1], 0.f);
#pragma unroll
  for (int i = 0; i < 4; ++i) { s0 = dot2(r.a[i], r.a[i], s0); s1 = dot2(r.b[i], r.b[i], s1); }
  return __builtin_amdgcn_rcpf(__builtin_amdgcn_sqrtf(s0 + s1) + 1e-6f);
}
DI unsigned scl(unsigned x, float s) { return pk(blo(x) * s, bhi(x) * s); }

DI void xform_k(unsigned base, int lane) {
  Row r;
  const unsigned src = base + lane * 40;
  u32x2 x0 = *LDSP(u32x2, src), x1 = *LDSP(u32x2, src + 8), x2 = *LDSP(u32x2, src + 16), x3 = *LDSP(u32x2, src + 24);
  r.c = *LDSP(u32x2, src + 32);
  r.a = __builtin_shufflevector(x0, x1, 0, 1, 2, 3); r.b = __builtin_shufflevector(x2, x3, 0, 1, 2, 3);
  const float s = inv_norm(r);
#pragma unroll
  for (int i = 0; i < 4; ++i) { r.a[i] = scl(r.a[i], s); r.b[i] = scl(r.b[i], s); }
  r.c[0] = scl(r.c[0], s); r.c[1] = scl(r.c[1], s);
  const unsigned m = base + (lane >> 5) * 1024 + (lane & 31) * 16;
  *LDSP(u32x4, m) = r.a;
  *LDSP(u32x4, m + 512) = r.b;
  *LDSP(u32x2, base + 2048 + lane * 8) = r.c;
}
DI void xform_v(unsigned base, int lane) {
  const unsigned src = base + 2560 + ((lane >> 5) * 8 + ((lane >> 2) & 3)) * 40 + ((lane & 16) ? 32 : (lane & 3) * 8);
  const int d = lane & 31;
  const unsigned dst = base + 2560 + (lane >> 5) * 320 + (d < 14 ? d + 6 : d - 14) * 16;
  s16x4 lo[4], hi[4];
#pragma unroll
  for (int g = 0; g < 4; ++g) {
    lo[g] = __builtin_amdgcn_ds_read_tr16_b64_v4i16(LDSP(s16x4, src + g * 640));
    hi[g] = __builtin_amdgcn_ds_read_tr16_b64_v4i16(LDSP(s16x4, src + g * 640 + 160));
  }
  if (d < 20) {
#pragma unroll
    for (int g = 0; g < 4; ++g) *LDSP(s16x8, dst + g * 640) = __builtin_shufflevector(lo[g], hi[g], 0, 1, 2, 3, 4, 5, 6, 7);
  }
}

__global__ __launch_bounds__(768, 3) void qknorm_attn(const unsigned* __restrict__ Q, const unsigned* __restrict__ K,
                                                      const unsigned* __restrict__ V, const unsigned* __restrict__ T,
                                                      unsigned* __restrict__ O) {
  unsigned tid = threadIdx.x, tb;
  const int w = __builtin_amdgcn_readfirstlane(tid >> 6), bx = blockIdx.x;
  asm volatile(
      "s_load_dwordx8 s[40:47], %[kp], 0\n\t"
      "s_load_dwordx2 s[48:49], %[kp], 32\n\t"
      "v_readfirstlane_b32 s64, v164\n\t"
      "v_and_b32 v164, 63, v164\n\t"
      "s_and_b32 s61, %[bx], 7\n\t"
      "s_lshr_b32 s65, %[bx], 5\n\t"
      "s_lshl3_add_u32 s65, s61, s65\n\t"
      "s_lshr_b32 s64, s64, 6\n\t"
      "s_mov_b32 s72, 153600\n\t"
      "s_mul_i32 s66, s65, 81920\n\t"
      "v_lshl_add_u32 v100, v164, 2, s72\n\t"
      "v_mov_b32 v101, 0\n\t"
      "ds_write_b32 v100, v101\n\t"
      "s_cmp_lg_u32 s64, 3\n\t"
      "s_cbranch_scc1 2f\n\t"
      "v_mul_u32_u24 v102, 64, v164\n\t"
      "s_getpc_b64 s[58:59]\n\t"
      "s_mov_b32 m0, 154624\n\t"
      "s_nop 0\n\t"
      "global_load_lds_dword v102, s[58:59]\n\t"
      "2:\n\t"
      "s_and_b32 s61, %[bx], 24\n\t"
      "s_add_u32 s67, s61, s64\n\t"
      "s_add_u32 s68, s64, 44\n\t"
      "s_and_b32 s70, s64, 2\n\t"
      "s_sub_u32 s61, s64, 8\n\t"
      "s_add_u32 s70, s70, s61\n\t"
      "s_cmp_ge_u32 s64, 8\n\t"
      "s_cselect_b32 s67, s61, s67\n\t"
      "s_cselect_b32 s68, s70, s68\n\t"
      "s_mulk_i32 s67, 2560\n\t"
      "s_mulk_i32 s68, 2560\n\t"
      "s_mov_b32 m0, s68\n\t"
      "v_lshl_add_u32 v89, v164, 4, s67\n\t"
      "s_and_b32 s70, s65, 30\n\t"
      "v_add_u32 v89, s66, v89\n\t"
      "s_lshl_b32 s70, s70, 1\n\t"
      "s_cmp_ge_u32 s64, 8\n\t"
      "s_waitcnt lgkmcnt(0)\n\t"
      "s_cbranch_scc1 11f\n\t"
      "s_load_dword %[tb], s[46:47], s70\n\t"
      "s_barrier\n\t"
      "s_cmp_lt_u32 s64, 4\n\t"
      "s_cbranch_scc1 12f\n\t"
      "s_sleep 8\n\t"
      "12:\n\t"
      "global_load_lds_dwordx4 v89, s[40:41]\n\t"
      "global_load_lds_dwordx4 v89, s[40:41] offset:1024\n\t"
      "s_mov_b32 exec_hi, 0\n\t"
      "global_load_lds_dwordx4 v89, s[40:41] offset:2048\n\t"
      "s_mov_b32 exec_hi, -1\n\t"
      "s_branch 10f\n\t"
      "11:\n\t"
      "global_load_lds_dwordx4 v89, s[42:43]\n\t"
      "global_load_lds_dwordx4 v89, s[42:43] offset:1024\n\t"
      "s_mov_b32 exec_hi, 0\n\t"
      "global_load_lds_dwordx4 v89, s[42:43] offset:2048\n\t"
      "s_add_u32 m0, m0, 5120\n\t"
      "s_setprio 3\n\t"
      "global_load_lds_dwordx4 v89, s[44:45] offset:2048\n\t"
      "s_mov_b32 exec_hi, -1\n\t"
      "global_load_lds_dwordx4 v89, s[44:45]\n\t"
      "global_load_lds_dwordx4 v89, s[44:45] offset:1024\n\t"
      "s_barrier\n\t"
      "s_mov_b32 s50, 0xaaaaaaaa\n\t"
      "s_mov_b32 s51, s50\n\t"
      "s_mov_b32 s52, 0xcccccccc\n\t"
      "s_mov_b32 s53, s52\n\t"
      "v_mov_b32 v100, 0x3f80\n\t"
      "v_cndmask_b32 v100, v100, 1.0, s[50:51]\n\t"
      "v_cndmask_b32 v160, v100, 0, s[52:53]\n\t"
      "v_cndmask_b32 v161, 0, v100, s[52:53]\n\t"
      "v_and_b32 v100, 31, v164\n\t"
      "v_lshrrev_b32 v101, 5, v164\n\t"
      "s_movk_i32 s61, 320\n\t"
      "s_andn2_b32 s69, 1, s65\n\t"
      "s_lshl_b32 s69, s69, 4\n\t"
      "s_mov_b32 s74, 0x41300000\n\t"
      "s_mov_b32 s54, 0xfffff\n\t"
      "s_mov_b32 s55, s54\n\t"
      "v_mad_u32_u24 v84, v164, 40, s68\n\t"
      "v_mul_u32_u24 v102, s61, v101\n\t"
      "v_add_u32 v102, s68, v102\n\t"
      "v_bfe_u32 v103, v164, 2, 2\n\t"
      "v_mad_u32_u24 v85, v103, 40, v102\n\t"
      "v_and_b32 v103, 3, v164\n\t"
      "v_lshlrev_b32 v103, 3, v103\n\t"
      "s_mov_b32 vcc_lo, 0xffff0000\n\t"
      "s_mov_b32 vcc_hi, vcc_lo\n\t"
      "v_cndmask_b32 v103, v103, 32, vcc\n\t"
      "v_add_u32 v85, v85, v103\n\t"
      "s_bitcmp1_b32 s64, 0\n\t"
      "s_cselect_b32 s62, 512, 0\n\t"
      "s_cselect_b32 s60, 0, 2048\n\t"
      "s_add_u32 s62, s62, s68\n\t"
      "s_add_u32 s60, s60, s68\n\t"
      "v_lshl_add_u32 v88, v101, 3, s60\n\t"
      "v_lshl_add_u32 v88, v100, 4, v88\n\t"
      "v_lshl_add_u32 v86, v101, 10, s62\n\t"
      "v_lshl_add_u32 v86, v100, 4, v86\n\t"
      "v_cmp_gt_u32 vcc, 14, v100\n\t"
      "s_nop 1\n\t"
      "v_cndmask_b32 v103, -14, 6, vcc\n\t"
      "v_add_u32 v103, v103, v100\n\t"
      "v_lshl_add_u32 v87, v103, 4, v102\n\t"
      "v_add_u32 v89, 10240, v89\n\t"
      "s_add_u32 s63, s68, 20480\n\t"
      "s_lshl2_add_u32 s61, s64, s72\n\t"
      "s_sub_u32 s61, s61, 32\n\t"
      "v_mov_b32 v153, s61\n\t"
      "v_mov_b32 v155, s72\n\t"
      "s_cmp_ge_u32 s64, 10\n\t"
      "s_cselect_b32 s71, s72, 0\n\t"
      "s_sub_u32 s71, 20480, s71\n\t"
      "s_mov_b32 s60, 0\n\t"
      "s_movk_i32 s62, 20480\n\t"
      "s_waitcnt vmcnt(0)\n\t"
      "s_branch 8f\n\t"
      "5:\n\t"
      "s_cmp_gt_u32 s60, 5\n\t"
      "s_cbranch_scc1 6f\n\t"
      "s_cmp_eq_u32 s60, 3\n\t"
      "s_cbranch_scc0 4f\n\t"
      "3:\n\t"
      "ds_read_b32 v154, v155 offset:16\n\t"
      "s_waitcnt lgkmcnt(0)\n\t"
      "v_readfirstlane_b32 s61, v154\n\t"
      "s_nop 3\n\t"
      "s_cmp_eq_u32 s61, 8\n\t"
      "s_cbranch_scc1 4f\n\t"
      "s_sleep 8\n\t"
      "s_branch 3b\n\t"
      "4:\n\t"
      "s_cmp_lt_u32 s63, s72\n\t"
      "s_cselect_b32 s61, 0, s72\n\t"
      "s_sub_u32 s63, s63, s61\n\t"
      "s_call_b64 s[56:57], 30f\n\t"
      "s_waitcnt vmcnt(12)\n\t"
      "s_branch 8f\n\t"
      "6:\n\t"
      "s_waitcnt vmcnt(6)\n\t"
      "s_cmp_eq_u32 s60, 6\n\t"
      "s_cbranch_scc1 8f\n\t"
      "s_waitcnt vmcnt(0)\n\t"
      "8:\n\t"
      "ds_read_b64 v[32:33], v84 offset:0\n\t"
      "ds_read_b64 v[34:35], v84 offset:8\n\t"
      "ds_read_b64 v[36:37], v84 offset:16\n\t"
      "ds_read_b64 v[38:39], v84 offset:24\n\t"
      "ds_read_b64 v[40:41], v84 offset:32\n\t"
      "ds_read_b64_tr_b16 v[68:69], v85 offset:5120\n\t"
      "ds_read_b64_tr_b16 v[70:71], v85 offset:5280\n\t"
      "ds_read_b64_tr_b16 v[72:73], v85 offset:5760\n\t"
      "ds_read_b64_tr_b16 v[74:75], v85 offset:5920\n\t"
      "ds_read_b64_tr_b16 v[76:77], v85 offset:6400\n\t"
      "ds_read_b64_tr_b16 v[78:79], v85 offset:6560\n\t"
      "ds_read_b64_tr_b16 v[80:81], v85 offset:7040\n\t"
      "ds_read_b64_tr_b16 v[82:83], v85 offset:7200\n\t"
      "s_waitcnt lgkmcnt(8)\n\t"
      "v_mfma_f32_4x4x4_16b_bf16 v[44:47], v[32:33], v[32:33], 0\n\t"
      "v_mfma_f32_4x4x4_16b_bf16 v[48:51], v[160:161], v[32:33], 0\n\t"
      "s_nop 0\n\t"
      "v_mfma_f32_4x4x4_16b_bf16 v[44:47], v[34:35], v[34:35], v[44:47]\n\t"
      "v_mfma_f32_4x4x4_16b_bf16 v[52:55], v[160:161], v[34:35], 0\n\t"
      "s_nop 0\n\t"
      "v_mfma_f32_4x4x4_16b_bf16 v[44:47], v[36:37], v[36:37], v[44:47]\n\t"
      "v_mfma_f32_4x4x4_16b_bf16 v[56:59], v[160:161], v[36:37], 0\n\t"
      "s_nop 0\n\t"
      "v_mfma_f32_4x4x4_16b_bf16 v[44:47], v[38:39], v[38:39], v[44:47]\n\t"
      "v_mfma_f32_4x4x4_16b_bf16 v[60:63], v[160:161], v[38:39], 0\n\t"
      "s_nop 0\n\t"
      "v_mfma_f32_4x4x4_16b_bf16 v[44:47], v[40:41], v[40:41], v[44:47]\n\t"
      "v_mfma_f32_4x4x4_16b_bf16 v[64:67], v[160:161], v[40:41], 0\n\t"
      "s_nop 3\n\t"
      "v_cndmask_b32 v44, v44, v45, s[50:51]\n\t"
      "v_cndmask_b32 v46, v46, v47, s[50:51]\n\t"
      "v_cndmask_b32 v44, v44, v46, s[52:53]\n\t"
      "v_sqrt_f32 v44, v44\n\t"
      "s_waitcnt lgkmcnt(0)\n\t"
      "s_mov_b64 exec, s[54:55]\n\t"
      "ds_write_b128 v87, v[68:71] offset:5120\n\t"
      "ds_write_b128 v87, v[72:75] offset:5760\n\t"
      "ds_write_b128 v87, v[76:79] offset:6400\n\t"
      "ds_write_b128 v87, v[80:83] offset:7040\n\t"
      "s_mov_b64 exec, -1\n\t"
      "v_add_f32 v44, 0x358637bd, v44\n\t"
      "v_rcp_f32 v44, v44\n\t"
      "s_nop 0\n\t"
      "v_mul_f32 v48, v48, v44\n\t"
      "v_mul_f32 v49, v49, v44\n\t"
      "v_mul_f32 v50, v50, v44\n\t"
      "v_mul_f32 v51, v51, v44\n\t"
      "v_cvt_pk_bf16_f32 v32, v48, v49\n\t"
      "v_mul_f32 v52, v52, v44\n\t"
      "v_mul_f32 v53, v53, v44\n\t"
      "v_cvt_pk_bf16_f32 v33, v50, v51\n\t"
      "v_mul_f32 v54, v54, v44\n\t"
      "v_mul_f32 v55, v55, v44\n\t"
      "v_cvt_pk_bf16_f32 v34, v52, v53\n\t"
      "v_mul_f32 v56, v56, v44\n\t"
      "v_mul_f32 v57, v57, v44\n\t"
      "v_cvt_pk_bf16_f32 v35, v54, v55\n\t"
      "ds_write_b128 v86, v[32:35] offset:0\n\t"
      "v_mul_f32 v58, v58, v44\n\t"
      "v_mul_f32 v59, v59, v44\n\t"
      "v_cvt_pk_bf16_f32 v36, v56, v57\n\t"
      "v_mul_f32 v60, v60, v44\n\t"
      "v_mul_f32 v61, v61, v44\n\t"
      "v_cvt_pk_bf16_f32 v37, v58, v59\n\t"
      "v_mul_f32 v62, v62, v44\n\t"
      "v_mul_f32 v63, v63, v44\n\t"
      "v_cvt_pk_bf16_f32 v38, v60, v61\n\t"
      "v_mul_f32 v64, v64, v44\n\t"
      "v_mul_f32 v65, v65, v44\n\t"
      "v_cvt_pk_bf16_f32 v39, v62, v63\n\t"
      "ds_write_b128 v86, v[36:39] offset:512\n\t"
      "v_mul_f32 v66, v66, v44\n\t"
      "v_mul_f32 v67, v67, v44\n\t"
      "v_cvt_pk_bf16_f32 v40, v64, v65\n\t"
      "v_cvt_pk_bf16_f32 v41, v66, v67\n\t"
      "ds_write_b64 v88, v[40:41]\n\t"
      "s_lshl1_add_u32 s61, s60, 1\n\t"
      "s_cmp_eq_u32 s60, 7\n\t"
      "s_cselect_b32 s61, 16, s61\n\t"
      "v_mov_b32 v154, s61\n\t"
      "s_waitcnt lgkmcnt(0)\n\t"
      "ds_write_b32 v153, v154\n\t"
      "s_cmp_eq_u32 s60, 6\n\t"
      "s_cselect_b32 s62, s71, s62\n\t"
      "v_add_u32 v84, s62, v84\n\t"
      "v_add_u32 v85, s62, v85\n\t"
      "v_add_u32 v86, s62, v86\n\t"
      "v_add_u32 v87, s62, v87\n\t"
      "v_add_u32 v88, s62, v88\n\t"
      "s_cmp_eq_u32 s60, 0\n\t"
      "s_cbranch_scc0 9f\n\t"
      "s_load_dword %[tb], s[46:47], s70\n\t"
      "s_call_b64 s[56:57], 30f\n\t"
      "s_call_b64 s[56:57], 30f\n\t"
      "s_waitcnt lgkmcnt(0)\n\t"
      "s_lshl_b32 %[tb], %[tb], s69\n\t"
      "s_and_b32 %[tb], %[tb], 0xffff0000\n\t"
      "s_and_b32 s61, %[tb], 0x7fffffff\n\t"
      "s_cmp_gt_u32 s61, s74\n\t"
      "s_cbranch_scc1 98f\n\t"
      "9:\n\t"
      "s_cmp_eq_u32 s60, 3\n\t"
      "s_cbranch_scc0 7f\n\t"
      "s_setprio 0\n\t"
      "7:\n\t"
      "s_add_u32 s60, s60, 1\n\t"
      "s_cmp_lt_u32 s60, 8\n\t"
      "s_cbranch_scc1 5b\n\t"
      "s_branch 98f\n\t"
      "30:\n\t"
      "s_mov_b32 m0, s63\n\t"
      "s_add_u32 s63, s63, 20480\n\t"
      "global_load_lds_dwordx4 v89, s[42:43]\n\t"
      "global_load_lds_dwordx4 v89, s[42:43] offset:1024\n\t"
      "s_mov_b32 exec_hi, 0\n\t"
      "global_load_lds_dwordx4 v89, s[42:43] offset:2048\n\t"
      "s_add_u32 m0, m0, 5120\n\t"
      "s_nop 0\n\t"
      "global_load_lds_dwordx4 v89, s[44:45] offset:2048\n\t"
      "s_mov_b32 exec_hi, -1\n\t"
      "global_load_lds_dwordx4 v89, s[44:45]\n\t"
      "global_load_lds_dwordx4 v89, s[44:45] offset:1024\n\t"
      "v_add_u32 v89, 10240, v89\n\t"
      "s_setpc_b64 s[56:57]\n\t"
      "98:\n\t"
      "s_waitcnt vmcnt(0) lgkmcnt(0)\n\t"
      "s_endpgm\n\t"
      "10:\n\t"
      "s_mov_b32 s50, 0xaaaaaaaa\n\t"
      "s_mov_b32 s51, s50\n\t"
      "s_mov_b32 s52, 0xcccccccc\n\t"
      "s_mov_b32 s53, s52\n\t"
      "v_mov_b32 v100, 0x3f80\n\t"
      "v_cndmask_b32 v100, v100, 1.0, s[50:51]\n\t"
      "v_cndmask_b32 v160, v100, 0, s[52:53]\n\t"
      "v_cndmask_b32 v161, 0, v100, s[52:53]\n\t"
      "v_and_b32 v100, 31, v164\n\t"
      "v_lshrrev_b32 v101, 5, v164\n\t"
      "s_movk_i32 s61, 320\n\t"
      "s_andn2_b32 s69, 1, s65\n\t"
      "s_lshl_b32 s69, s69, 4\n\t"
      "s_mov_b32 s74, 0x41300000\n\t"
      "s_mov_b32 s54, 0x33ffff\n\t"
      "s_mov_b32 s55, s54\n\t"
      "v_mad_u32_u24 v162, v164, 40, s68\n\t"
      "v_lshlrev_b32 v123, 4, v164\n\t"
      "v_mov_b32 v153, s72\n\t"
      "v_mul_u32_u24 v102, s61, v101\n\t"
      "s_add_u32 s67, s67, s66\n\t"
      "v_mad_u32_u24 v152, v100, 40, s67\n\t"
      "v_mad_u32_u24 v152, v101, 20, v152\n\t"
      "v_lshrrev_b32 v101, 1, v164\n\t"
      "v_and_b32 v103, 3, v164\n\t"
      "v_and_or_b32 v101, v101, 12, v103\n\t"
      "v_bfe_u32 v103, v164, 2, 1\n\t"
      "v_mad_u32_u24 v101, v103, 10, v101\n\t"
      "v_cmp_gt_u32 vcc, 14, v101\n\t"
      "s_nop 1\n\t"
      "v_cndmask_b32 v103, -14, 6, vcc\n\t"
      "v_add_u32 v103, v103, v101\n\t"
      "v_lshl_add_u32 v124, v103, 4, v102\n\t"
      "s_mov_b32 vcc_lo, 0x440000\n\t"
      "s_mov_b32 vcc_hi, vcc_lo\n\t"
      "v_mov_b32 v103, 0x3f803f80\n\t"
      "v_cndmask_b32 v158, 0, v103, vcc\n\t"
      "v_cndmask_b32 v159, 0, v103, vcc\n\t"
      "s_movk_i32 s73, 10240\n\t"
      "s_cmp_ge_u32 s64, 4\n\t"
      "s_mov_b32 s71, 0x1d99b\n\t"
      "s_cmov_b32 s71, 0x1c003\n\t"
      "s_mov_b32 s56, 0x88880000\n\t"
      "s_cmov_b32 s56, 0x80000000\n\t"
      "s_mov_b32 s57, 0x11110000\n\t"
      "s_cmov_b32 s57, 0x1\n\t"
      "v_mov_b64 v[88:89], 0\n\t"
      "v_mov_b64 v[90:91], 0\n\t"
      "v_mov_b64 v[92:93], 0\n\t"
      "v_mov_b64 v[94:95], 0\n\t"
      "v_mov_b32 v122, 0xdddddddd\n\t"
      "v_mov_b32 v163, 0x3fb8aa3b\n\t"
      "v_mfma_f32_32x32x4_2b_bf16 v[0:31], v[88:89], v[88:89], 0\n\t"
      "v_mov_b64 v[136:137], v[158:159]\n\t"
      "v_mov_b64 v[138:139], v[158:159]\n\t"
      "v_mov_b64 v[140:141], v[158:159]\n\t"
      "v_mov_b64 v[142:143], v[158:159]\n\t"
      "v_mov_b64 v[144:145], v[158:159]\n\t"
      "v_mov_b64 v[146:147], v[158:159]\n\t"
      "v_mov_b64 v[148:149], v[158:159]\n\t"
      "v_mov_b64 v[150:151], v[158:159]\n\t"
      "s_waitcnt lgkmcnt(0)\n\t"
      "s_lshl_b32 %[tb], %[tb], s69\n\t"
      "s_and_b32 %[tb], %[tb], 0xffff0000\n\t"
      "v_mul_f32 v163, %[tb], v163\n\t"
      "v_mov_b32 v96, 0xc1ac0000\n\t"
      "v_mov_b32 v97, 0xc1ac0000\n\t"
      "v_mov_b64 v[98:99], v[96:97]\n\t"
      "v_mov_b64 v[100:101], v[96:97]\n\t"
      "v_mov_b64 v[102:103], v[96:97]\n\t"
      "v_mov_b64 v[104:105], v[96:97]\n\t"
      "v_mov_b64 v[106:107], v[96:97]\n\t"
      "v_mov_b64 v[108:109], v[96:97]\n\t"
      "v_mov_b64 v[110:111], v[96:97]\n\t"
      "s_and_b32 s61, %[tb], 0x7fffffff\n\t"
      "s_cmp_gt_u32 s61, s74\n\t"
      "s_cbranch_scc1 99f\n\t"
      "s_mov_b32 s60, 0\n\t"
      "s_movk_i32 s62, 10240\n\t"
      "s_waitcnt vmcnt(0)\n\t"
      "ds_read_b64 v[32:33], v162 offset:0\n\t"
      "ds_read_b64 v[34:35], v162 offset:8\n\t"
      "ds_read_b64 v[36:37], v162 offset:16\n\t"
      "ds_read_b64 v[38:39], v162 offset:24\n\t"
      "ds_read_b64 v[40:41], v162 offset:32\n\t"
      "s_waitcnt lgkmcnt(0)\n\t"
      "v_mfma_f32_4x4x4_16b_bf16 v[44:47], v[32:33], v[32:33], 0\n\t"
      "v_mfma_f32_4x4x4_16b_bf16 v[48:51], v[160:161], v[32:33], 0\n\t"
      "s_nop 0\n\t"
      "v_mfma_f32_4x4x4_16b_bf16 v[44:47], v[34:35], v[34:35], v[44:47]\n\t"
      "v_mfma_f32_4x4x4_16b_bf16 v[52:55], v[160:161], v[34:35], 0\n\t"
      "s_nop 0\n\t"
      "v_mfma_f32_4x4x4_16b_bf16 v[44:47], v[36:37], v[36:37], v[44:47]\n\t"
      "v_mfma_f32_4x4x4_16b_bf16 v[56:59], v[160:161], v[36:37], 0\n\t"
      "s_nop 0\n\t"
      "v_mfma_f32_4x4x4_16b_bf16 v[44:47], v[38:39], v[38:39], v[44:47]\n\t"
      "v_mfma_f32_4x4x4_16b_bf16 v[60:63], v[160:161], v[38:39], 0\n\t"
      "s_nop 0\n\t"
      "v_mfma_f32_4x4x4_16b_bf16 v[44:47], v[40:41], v[40:41], v[44:47]\n\t"
      "v_mfma_f32_4x4x4_16b_bf16 v[64:67], v[160:161], v[40:41], 0\n\t"
      "s_nop 3\n\t"
      "v_cndmask_b32 v44, v44, v45, s[50:51]\n\t"
      "v_cndmask_b32 v46, v46, v47, s[50:51]\n\t"
      "v_cndmask_b32 v44, v44, v46, s[52:53]\n\t"
      "v_sqrt_f32 v44, v44\n\t"
      "s_nop 0\n\t"
      "v_add_f32 v44, 0x358637bd, v44\n\t"
      "v_rcp_f32 v44, v44\n\t"
      "s_nop 0\n\t"
      "v_mul_f32 v44, v163, v44\n\t"
      "v_mul_f32 v48, v48, v44\n\t"
      "v_mul_f32 v49, v49, v44\n\t"
      "v_mul_f32 v50, v50, v44\n\t"
      "v_mul_f32 v51, v51, v44\n\t"
      "v_mul_f32 v52, v52, v44\n\t"
      "v_mul_f32 v53, v53, v44\n\t"
      "v_mul_f32 v54, v54, v44\n\t"
      "v_mul_f32 v55, v55, v44\n\t"
      "v_mul_f32 v56, v56, v44\n\t"
      "v_mul_f32 v57, v57, v44\n\t"
      "v_mul_f32 v58, v58, v44\n\t"
      "v_mul_f32 v59, v59, v44\n\t"
      "v_mul_f32 v60, v60, v44\n\t"
      "v_mul_f32 v61, v61, v44\n\t"
      "v_mul_f32 v62, v62, v44\n\t"
      "v_mul_f32 v63, v63, v44\n\t"
      "v_mul_f32 v64, v64, v44\n\t"
      "v_mul_f32 v65, v65, v44\n\t"
      "v_mul_f32 v66, v66, v44\n\t"
      "v_mul_f32 v67, v67, v44\n\t"
      "v_cvt_pk_bf16_f32 v112, v48, v49\n\t"
      "v_cvt_pk_bf16_f32 v113, v50, v51\n\t"
      "v_cvt_pk_bf16_f32 v114, v52, v53\n\t"
      "v_cvt_pk_bf16_f32 v115, v54, v55\n\t"
      "v_cvt_pk_bf16_f32 v116, v56, v57\n\t"
      "v_cvt_pk_bf16_f32 v117, v58, v59\n\t"
      "v_cvt_pk_bf16_f32 v118, v60, v61\n\t"
      "v_cvt_pk_bf16_f32 v119, v62, v63\n\t"
      "v_cvt_pk_bf16_f32 v120, v64, v65\n\t"
      "v_cvt_pk_bf16_f32 v121, v66, v67\n\t"
      "v_permlane32_swap_b32 v112, v116\n\t"
      "v_permlane32_swap_b32 v113, v117\n\t"
      "v_permlane32_swap_b32 v114, v118\n\t"
      "v_permlane32_swap_b32 v115, v119\n\t"
      "33:\n\t"
      "ds_read_b128 v[154:157], v153\n\t"
      "s_waitcnt lgkmcnt(0)\n\t"
      "v_min3_u32 v154, v154, v155, v156\n\t"
      "v_min_u32 v154, v154, v157\n\t"
      "s_nop 0\n\t"
      "v_readfirstlane_b32 s63, v154\n\t"
      "s_nop 3\n\t"
      "s_cmp_ge_u32 s60, s63\n\t"
      "s_cbranch_scc1 33b\n\t"
      "ds_read_b128 v[128:131], v123 offset:0\n\t"
      "ds_read_b128 v[132:135], v123 offset:2048\n\t"
      "s_mov_b64 exec, s[54:55]\n\t"
      "ds_read_b128 v[136:139], v124 offset:5120\n\t"
      "ds_read_b128 v[140:143], v124 offset:5760\n\t"
      "s_mov_b64 exec, -1\n\t"
      "s_waitcnt lgkmcnt(3)\n\t"
      "v_mfma_f32_32x32x16_bf16 v[32:47], v[128:131], v[112:115], v[96:111]\n\t"
      "v_mfma_f32_32x32x16_bf16 v[48:63], v[128:131], v[116:119], v[96:111]\n\t"
      "ds_read_b128 v[128:131], v123 offset:1024\n\t"
      "s_waitcnt lgkmcnt(3)\n\t"
      "s_nop 8\n\t"
      "v_mfma_f32_32x32x4_2b_bf16 v[32:63], v[132:133], v[120:121], v[32:63] cbsz:1 abid:0\n\t"
      "s_waitcnt lgkmcnt(0)\n\t"
      "v_mfma_f32_32x32x16_bf16 v[64:79], v[128:131], v[112:115], v[96:111]\n\t"
      "s_nop 15\n\t"
      "s_nop 3\n\t"
      "s_branch 21f\n\t"
      "20:\n\t"
      "v_exp_f32 v32, v32\n\t"
      "v_exp_f32 v33, v33\n\t"
      "v_smfmac_f32_32x32x32_bf16 v[16:31], v[148:151], v[88:95], v122\n\t"
      "s_mov_b64 exec, s[54:55]\n\t"
      "ds_read_b128 v[144:147], v124 offset:6400\n\t"
      "ds_read_b128 v[148:151], v124 offset:7040\n\t"
      "s_mov_b64 exec, -1\n\t"
      "v_exp_f32 v34, v34\n\t"
      "v_exp_f32 v35, v35\n\t"
      "v_exp_f32 v36, v36\n\t"
      "v_exp_f32 v37, v37\n\t"
      "v_mfma_f32_32x32x16_bf16 v[80:95], v[128:131], v[116:119], v[96:111]\n\t"
      "ds_read_b128 v[128:131], v123 offset:3072\n\t"
      "v_exp_f32 v38, v38\n\t"
      "v_exp_f32 v39, v39\n\t"
      "v_exp_f32 v40, v40\n\t"
      "v_exp_f32 v41, v41\n\t"
      "v_smfmac_f32_32x32x32_bf16 v[0:15], v[136:139], v[32:39], v122\n\t"
      "v_exp_f32 v42, v42\n\t"
      "v_exp_f32 v43, v43\n\t"
      "v_exp_f32 v44, v44\n\t"
      "v_exp_f32 v45, v45\n\t"
      "v_exp_f32 v46, v46\n\t"
      "v_exp_f32 v47, v47\n\t"
      "v_mfma_f32_32x32x4_2b_bf16 v[64:95], v[134:135], v[120:121], v[64:95] cbsz:1 abid:0\n\t"
      "v_exp_f32 v48, v48\n\t"
      "v_exp_f32 v49, v49\n\t"
      "v_exp_f32 v50, v50\n\t"
      "v_exp_f32 v51, v51\n\t"
      "v_exp_f32 v52, v52\n\t"
      "v_exp_f32 v53, v53\n\t"
      "v_smfmac_f32_32x32x32_bf16 v[0:15], v[140:143], v[40:47], v122\n\t"
      "v_exp_f32 v54, v54\n\t"
      "v_exp_f32 v55, v55\n\t"
      "v_exp_f32 v56, v56\n\t"
      "v_exp_f32 v57, v57\n\t"
      "v_smfmac_f32_32x32x32_bf16 v[16:31], v[136:139], v[48:55], v122\n\t"
      "v_exp_f32 v58, v58\n\t"
      "v_exp_f32 v59, v59\n\t"
      "v_exp_f32 v60, v60\n\t"
      "v_exp_f32 v61, v61\n\t"
      "s_waitcnt lgkmcnt(0)\n\t"
      "v_mfma_f32_32x32x16_bf16 v[32:47], v[128:131], v[112:115], v[96:111]\n\t"
      "v_exp_f32 v62, v62\n\t"
      "v_exp_f32 v63, v63\n\t"
      "v_exp_f32 v64, v64\n\t"
      "v_exp_f32 v65, v65\n\t"
      "v_smfmac_f32_32x32x32_bf16 v[16:31], v[140:143], v[56:63], v122\n\t"
      "s_mov_b64 exec, s[54:55]\n\t"
      "ds_read_b128 v[136:139], v124 offset:7680\n\t"
      "ds_read_b128 v[140:143], v124 offset:8320\n\t"
      "s_mov_b64 exec, -1\n\t"
      "v_exp_f32 v66, v66\n\t"
      "v_exp_f32 v67, v67\n\t"
      "v_exp_f32 v68, v68\n\t"
      "v_exp_f32 v69, v69\n\t"
      "v_mfma_f32_32x32x16_bf16 v[48:63], v[128:131], v[116:119], v[96:111]\n\t"
      "ds_read_b128 v[128:131], v123 offset:4096\n\t"
      "v_exp_f32 v70, v70\n\t"
      "v_exp_f32 v71, v71\n\t"
      "v_exp_f32 v72, v72\n\t"
      "v_exp_f32 v73, v73\n\t"
      "v_smfmac_f32_32x32x32_bf16 v[0:15], v[144:147], v[64:71], v122\n\t"
      "v_exp_f32 v74, v74\n\t"
      "v_exp_f32 v75, v75\n\t"
      "v_exp_f32 v76, v76\n\t"
      "v_exp_f32 v77, v77\n\t"
      "v_exp_f32 v78, v78\n\t"
      "v_exp_f32 v79, v79\n\t"
      "v_mfma_f32_32x32x4_2b_bf16 v[32:63], v[132:133], v[120:121], v[32:63] cbsz:1 abid:1\n\t"
      "v_exp_f32 v80, v80\n\t"
      "v_exp_f32 v81, v81\n\t"
      "v_exp_f32 v82, v82\n\t"
      "v_exp_f32 v83, v83\n\t"
      "v_exp_f32 v84, v84\n\t"
      "v_exp_f32 v85, v85\n\t"
      "v_smfmac_f32_32x32x32_bf16 v[0:15], v[148:151], v[72:79], v122\n\t"
      "v_exp_f32 v86, v86\n\t"
      "v_exp_f32 v87, v87\n\t"
      "v_exp_f32 v88, v88\n\t"
      "v_exp_f32 v89, v89\n\t"
      "v_smfmac_f32_32x32x32_bf16 v[16:31], v[144:147], v[80:87], v122\n\t"
      "v_exp_f32 v90, v90\n\t"
      "v_exp_f32 v91, v91\n\t"
      "v_exp_f32 v92, v92\n\t"
      "v_exp_f32 v93, v93\n\t"
      "s_waitcnt lgkmcnt(0)\n\t"
      "v_mfma_f32_32x32x16_bf16 v[64:79], v[128:131], v[112:115], v[96:111]\n\t"
      "v_exp_f32 v94, v94\n\t"
      "v_exp_f32 v95, v95\n\t"
      "v_exp_f32 v32, v32\n\t"
      "v_exp_f32 v33, v33\n\t"
      "v_smfmac_f32_32x32x32_bf16 v[16:31], v[148:151], v[88:95], v122\n\t"
      "s_mov_b64 exec, s[54:55]\n\t"
      "ds_read_b128 v[144:147], v124 offset:8960\n\t"
      "ds_read_b128 v[148:151], v124 offset:9600\n\t"
      "s_mov_b64 exec, -1\n\t"
      "v_exp_f32 v34, v34\n\t"
      "v_exp_f32 v35, v35\n\t"
      "v_exp_f32 v36, v36\n\t"
      "v_exp_f32 v37, v37\n\t"
      "v_mfma_f32_32x32x16_bf16 v[80:95], v[128:131], v[116:119], v[96:111]\n\t"
      "s_cmp_ge_u32 s60, s63\n\t"
      "s_cbranch_scc1 35f\n\t"
      "1:\n\t"
      "v_add_u32 v123, s62, v123\n\t"
      "ds_read_b128 v[128:131], v123 offset:0\n\t"
      "v_exp_f32 v38, v38\n\t"
      "v_exp_f32 v39, v39\n\t"
      "v_exp_f32 v40, v40\n\t"
      "v_exp_f32 v41, v41\n\t"
      "v_smfmac_f32_32x32x32_bf16 v[0:15], v[136:139], v[32:39], v122\n\t"
      "v_exp_f32 v42, v42\n\t"
      "v_exp_f32 v43, v43\n\t"
      "v_exp_f32 v44, v44\n\t"
      "v_exp_f32 v45, v45\n\t"
      "v_exp_f32 v46, v46\n\t"
      "v_exp_f32 v47, v47\n\t"
      "v_mfma_f32_32x32x4_2b_bf16 v[64:95], v[134:135], v[120:121], v[64:95] cbsz:1 abid:1\n\t"
      "ds_read_b128 v[132:135], v123 offset:2048\n\t"
      "v_exp_f32 v48, v48\n\t"
      "v_exp_f32 v49, v49\n\t"
      "v_exp_f32 v50, v50\n\t"
      "v_exp_f32 v51, v51\n\t"
      "v_exp_f32 v52, v52\n\t"
      "v_exp_f32 v53, v53\n\t"
      "v_smfmac_f32_32x32x32_bf16 v[0:15], v[140:143], v[40:47], v122\n\t"
      "v_exp_f32 v54, v54\n\t"
      "v_exp_f32 v55, v55\n\t"
      "v_exp_f32 v56, v56\n\t"
      "v_exp_f32 v57, v57\n\t"
      "v_smfmac_f32_32x32x32_bf16 v[16:31], v[136:139], v[48:55], v122\n\t"
      "v_exp_f32 v58, v58\n\t"
      "v_exp_f32 v59, v59\n\t"
      "v_exp_f32 v60, v60\n\t"
      "v_exp_f32 v61, v61\n\t"
      "s_waitcnt lgkmcnt(1)\n\t"
      "v_mfma_f32_32x32x16_bf16 v[32:47], v[128:131], v[112:115], v[96:111]\n\t"
      "v_exp_f32 v62, v62\n\t"
      "v_exp_f32 v63, v63\n\t"
      "v_exp_f32 v64, v64\n\t"
      "v_exp_f32 v65, v65\n\t"
      "v_smfmac_f32_32x32x32_bf16 v[16:31], v[140:143], v[56:63], v122\n\t"
      "v_add_u32 v124, s62, v124\n\t"
      "s_mov_b64 exec, s[54:55]\n\t"
      "ds_read_b128 v[136:139], v124 offset:5120\n\t"
      "ds_read_b128 v[140:143], v124 offset:5760\n\t"
      "s_mov_b64 exec, -1\n\t"
      "v_exp_f32 v66, v66\n\t"
      "v_exp_f32 v67, v67\n\t"
      "v_exp_f32 v68, v68\n\t"
      "v_exp_f32 v69, v69\n\t"
      "v_mfma_f32_32x32x16_bf16 v[48:63], v[128:131], v[116:119], v[96:111]\n\t"
      "ds_read_b128 v[128:131], v123 offset:1024\n\t"
      "v_exp_f32 v70, v70\n\t"
      "v_exp_f32 v71, v71\n\t"
      "v_exp_f32 v72, v72\n\t"
      "v_exp_f32 v73, v73\n\t"
      "v_smfmac_f32_32x32x32_bf16 v[0:15], v[144:147], v[64:71], v122\n\t"
      "v_exp_f32 v74, v74\n\t"
      "v_exp_f32 v75, v75\n\t"
      "v_exp_f32 v76, v76\n\t"
      "v_exp_f32 v77, v77\n\t"
      "v_exp_f32 v78, v78\n\t"
      "v_exp_f32 v79, v79\n\t"
      "s_waitcnt lgkmcnt(3)\n\t"
      "v_mfma_f32_32x32x4_2b_bf16 v[32:63], v[132:133], v[120:121], v[32:63] cbsz:1 abid:0\n\t"
      "v_exp_f32 v80, v80\n\t"
      "v_exp_f32 v81, v81\n\t"
      "v_exp_f32 v82, v82\n\t"
      "v_exp_f32 v83, v83\n\t"
      "v_exp_f32 v84, v84\n\t"
      "v_exp_f32 v85, v85\n\t"
      "v_smfmac_f32_32x32x32_bf16 v[0:15], v[148:151], v[72:79], v122\n\t"
      "v_exp_f32 v86, v86\n\t"
      "v_exp_f32 v87, v87\n\t"
      "v_exp_f32 v88, v88\n\t"
      "v_exp_f32 v89, v89\n\t"
      "v_smfmac_f32_32x32x32_bf16 v[16:31], v[144:147], v[80:87], v122\n\t"
      "v_exp_f32 v90, v90\n\t"
      "v_exp_f32 v91, v91\n\t"
      "v_exp_f32 v92, v92\n\t"
      "v_exp_f32 v93, v93\n\t"
      "s_waitcnt lgkmcnt(0)\n\t"
      "v_mfma_f32_32x32x16_bf16 v[64:79], v[128:131], v[112:115], v[96:111]\n\t"
      "v_exp_f32 v94, v94\n\t"
      "v_exp_f32 v95, v95\n\t"
      "s_add_u32 s60, s60, 1\n\t"
      "s_bitcmp1_b32 s71, s60\n\t"
      "s_cbranch_scc0 20b\n\t"
      "s_bitcmp1_b32 s60, 4\n\t"
      "s_cbranch_scc1 22f\n\t"
      "21:\n\t"
      "s_add_u32 s61, s60, 0\n\t"
      "s_bitcmp0_b64 s[56:57], s61\n\t"
      "s_cbranch_scc1 60f\n\t"
      "s_setprio 0\n\t"
      "60:\n\t"
      "s_add_u32 s61, s60, 16\n\t"
      "s_bitcmp0_b64 s[56:57], s61\n\t"
      "s_cbranch_scc1 61f\n\t"
      "s_setprio 1\n\t"
      "61:\n\t"
      "s_add_u32 s61, s60, 32\n\t"
      "s_bitcmp0_b64 s[56:57], s61\n\t"
      "s_cbranch_scc1 62f\n\t"
      "s_setprio 2\n\t"
      "62:\n\t"
      "s_add_u32 s61, s60, 48\n\t"
      "s_bitcmp0_b64 s[56:57], s61\n\t"
      "s_cbranch_scc1 63f\n\t"
      "s_setprio 3\n\t"
      "63:\n\t"
      "s_cmp_eq_u32 s60, 14\n\t"
      "s_cselect_b32 s62, 0xfffdd000, s73\n\t"
      "s_cmp_eq_u32 s60, 1\n\t"
      "s_cbranch_scc0 20b\n\t"
      "v_mov_b32 v154, 1\n\t"
      "s_mov_b64 exec, 1\n\t"
      "ds_add_u32 v153, v154 offset:16\n\t"
      "s_mov_b64 exec, -1\n\t"
      "s_branch 20b\n\t"
      "34:\n\t"
      "s_sleep 2\n\t"
      "35:\n\t"
      "ds_read_b128 v[154:157], v153\n\t"
      "s_waitcnt lgkmcnt(0)\n\t"
      "v_min3_u32 v154, v154, v155, v156\n\t"
      "v_min_u32 v154, v154, v157\n\t"
      "s_nop 0\n\t"
      "v_readfirstlane_b32 s63, v154\n\t"
      "s_nop 3\n\t"
      "s_cmp_ge_u32 s60, s63\n\t"
      "s_cbranch_scc1 34b\n\t"
      "s_branch 1b\n\t"
      "22:\n\t"
      "s_nop 1\n\t"
      "v_smfmac_f32_32x32x32_bf16 v[16:31], v[148:151], v[88:95], v122\n\t"
      "v_rcp_f32 v44, v10\n\t"
      "s_nop 0\n\t"
      "v_mul_f32 v0, v0, v44\n\t"
      "v_mul_f32 v1, v1, v44\n\t"
      "v_mul_f32 v2, v2, v44\n\t"
      "v_mul_f32 v3, v3, v44\n\t"
      "v_mul_f32 v4, v4, v44\n\t"
      "v_mul_f32 v5, v5, v44\n\t"
      "v_mul_f32 v6, v6, v44\n\t"
      "v_mul_f32 v7, v7, v44\n\t"
      "v_mul_f32 v8, v8, v44\n\t"
      "v_mul_f32 v9, v9, v44\n\t"
      "v_cvt_pk_bf16_f32 v48, v0, v1\n\t"
      "v_cvt_pk_bf16_f32 v49, v2, v3\n\t"
      "v_cvt_pk_bf16_f32 v50, v4, v5\n\t"
      "v_cvt_pk_bf16_f32 v51, v6, v7\n\t"
      "v_cvt_pk_bf16_f32 v52, v8, v9\n\t"
      "global_store_dwordx4 v152, v[48:51], s[48:49] offset:0 nt\n\t"
      "global_store_dword v152, v52, s[48:49] offset:16 nt\n\t"
      "v_rcp_f32 v45, v26\n\t"
      "s_nop 0\n\t"
      "v_mul_f32 v16, v16, v45\n\t"
      "v_mul_f32 v17, v17, v45\n\t"
      "v_mul_f32 v18, v18, v45\n\t"
      "v_mul_f32 v19, v19, v45\n\t"
      "v_mul_f32 v20, v20, v45\n\t"
      "v_mul_f32 v21, v21, v45\n\t"
      "v_mul_f32 v22, v22, v45\n\t"
      "v_mul_f32 v23, v23, v45\n\t"
      "v_mul_f32 v24, v24, v45\n\t"
      "v_mul_f32 v25, v25, v45\n\t"
      "v_cvt_pk_bf16_f32 v58, v16, v17\n\t"
      "v_cvt_pk_bf16_f32 v59, v18, v19\n\t"
      "v_cvt_pk_bf16_f32 v60, v20, v21\n\t"
      "v_cvt_pk_bf16_f32 v61, v22, v23\n\t"
      "v_cvt_pk_bf16_f32 v62, v24, v25\n\t"
      "global_store_dwordx4 v152, v[58:61], s[48:49] offset:1280 nt\n\t"
      "global_store_dword v152, v62, s[48:49] offset:1296 nt\n\t"
      "99:\n\t"
      : "+{v164}"(tid), [tb] "=&s"(tb)
      : [kp] "s"(__builtin_amdgcn_kernarg_segment_ptr()), [bx] "s"(bx)
      : "memory", "scc", "vcc", "v0", "v1", "v2", "v3", "v4", "v5", "v6", "v7", "v8", "v9", "v10", "v11", "v12", "v13", "v14", "v15", "v16", "v17", "v18", "v19", "v20", "v21", "v22", "v23", "v24", "v25", "v26", "v27", "v28", "v29", "v30", "v31", "v32", "v33", "v34", "v35", "v36", "v37", "v38", "v39", "v40", "v41", "v42", "v43", "v44", "v45", "v46", "v47", "v48", "v49", "v50", "v51", "v52", "v53", "v54", "v55", "v56", "v57", "v58", "v59", "v60", "v61", "v62", "v63", "v64", "v65", "v66", "v67", "v68", "v69", "v70", "v71", "v72", "v73", "v74", "v75", "v76", "v77", "v78", "v79", "v80", "v81", "v82", "v83", "v84", "v85", "v86", "v87", "v88", "v89", "v90", "v91", "v92", "v93", "v94", "v95", "v96", "v97", "v98", "v99", "v100", "v101", "v102", "v103", "v104", "v105", "v106", "v107", "v108", "v109", "v110", "v111", "v112", "v113", "v114", "v115", "v116", "v117", "v118", "v119", "v120", "v121", "v122", "v123", "v124", "v125", "v126", "v127", "v128", "v129", "v130", "v131", "v132", "v133", "v134", "v135", "v136", "v137", "v138", "v139", "v140", "v141", "v142", "v143", "v144", "v145", "v146", "v147", "v148", "v149", "v150", "v151", "v152", "v153", "v154", "v155", "v156", "v157", "v158", "v159", "v160", "v161", "v162", "v163", "s40", "s41", "s42", "s43", "s44", "s45", "s46", "s47", "s48", "s49", "s50", "s51", "s52", "s53", "s54", "s55", "s56", "s57", "s58", "s59", "s60", "s61", "s62", "s63", "s64", "s65", "s66", "s67", "s68", "s69", "s70", "s71", "s72", "s73", "s74");
  if (__builtin_expect((tb & 0x7fffffffu) > T_SAFE_BITS, 0)) {
    const float tv = __uint_as_float(tb);
    int lane;
    asm volatile("s_waitcnt vmcnt(0)\n\ts_barrier\n\tv_mbcnt_lo_u32_b32 %0, -1, 0\n\tv_mbcnt_hi_u32_b32 %0, -1, %0" : "=v"(lane) :: "memory");
    const int r32 = lane & 31, hi = lane >> 5, dv = (r32 & 3) + 4 * (r32 >> 3) + 10 * ((r32 >> 2) & 1);
    const unsigned vaddr = hi * 320 + (dv < 14 ? dv + 6 : dv - 14) * 16;
    const unsigned one2 = (r32 == 18 || r32 == 22) ? 0x3f803f80u : 0u;
    const int bh = (bx & 7) * 8 + (bx >> 5), qb = (bx >> 3) & 3;
    const unsigned hoff = bh * (SEQ * DH / 2), qoff = hoff + (qb * 512 + w * 64) * (DH / 2);
    Q += qoff; K += hoff; V += hoff; O += qoff;
    const bool vact = (r32 < 18) || r32 == 20 || r32 == 21;
    const unsigned idx = 0xddddddddu;
    f32x16 o0, o1;
#pragma unroll
    for (int i = 0; i < 16; ++i) { o0[i] = 0.f; o1[i] = 0.f; }
    Row q;
    q.a = *(const u32x4a*)(Q + lane * (DH / 2)); q.b = *(const u32x4a*)(Q + lane * (DH / 2) + 4);
    q.c = *(const u32x2a*)(Q + lane * (DH / 2) + 8);
#pragma unroll 1
    for (int b = w; b < SEQ / 64; b += 8) {
      for (int i = lane; i < 160; i += 64) {
        *LDSP(u32x4, b * BLK + i * 16) = *(const u32x4*)(K + b * 64 * (DH / 2) + i * 4);
        *LDSP(u32x4, b * BLK + 2560 + i * 16) = *(const u32x4*)(V + b * 64 * (DH / 2) + i * 4);
      }
      xform_v(b * BLK, lane);
      xform_k(b * BLK, lane);
    }
    const float s = tv < 0.f ? -inv_norm(q) : inv_norm(q);
    u32x4 qm0, qm1; u32x2 qt;
#pragma unroll
    for (int i = 0; i < 4; ++i) {
      auto x = __builtin_amdgcn_permlane32_swap(scl(q.a[i], s), scl(q.b[i], s), false, false); qm0[i] = x[0]; qm1[i] = x[1];
    }
    qt[0] = scl(q.c[0], s); qt[1] = scl(q.c[1], s);
    asm volatile("s_waitcnt lgkmcnt(0)\n\ts_barrier" ::: "memory");
    const float at = __builtin_fabsf(tv);
    float m0 = -INFINITY, m1 = -INFINITY;
    f32x16 z;
#pragma unroll
    for (int i = 0; i < 16; ++i) z[i] = 0.f;
#pragma unroll 1
    for (int t = 0; t < SEQ / 32; ++t) {
      const unsigned base = (t >> 1) * BLK;
      const bf16x8 ka = *LDSP(bf16x8, base + (t & 1) * 1024 + lane * 16);
      const s16x4 kt = *LDSP(s16x4, base + 2048 + ((t & 1) * 32 + r32) * 8);
      u32x4 v0 = {one2, one2, one2, one2}, v1 = v0;
      if (vact) { v0 = *LDSP(u32x4, base + 2560 + (t & 1) * 1280 + vaddr); v1 = *LDSP(u32x4, base + 2560 + (t & 1) * 1280 + 640 + vaddr); }
      f32x16 s0 = __builtin_amdgcn_mfma_f32_32x32x16_bf16(ka, __builtin_bit_cast(bf16x8, qm0), z, 0, 0, 0);
      f32x16 s1 = __builtin_amdgcn_mfma_f32_32x32x16_bf16(ka, __builtin_bit_cast(bf16x8, qm1), z, 0, 0, 0);
      f32x32 s = __builtin_shufflevector(s0, s1, 0, 1, 2, 3, 4, 5, 6, 7, 8, 9, 10, 11, 12, 13, 14, 15, 16, 17, 18, 19, 20, 21, 22, 23, 24, 25, 26, 27, 28, 29, 30, 31);
      s = __builtin_amdgcn_mfma_f32_32x32x4bf16_1k(kt, __builtin_bit_cast(s16x4, qt), s, 0, 0, 0);
      float x0 = s[0], x1 = s[16];
#pragma unroll
      for (int i = 1; i < 16; ++i) { x0 = __builtin_fmaxf(x0, s[i]); x1 = __builtin_fmaxf(x1, s[16 + i]); }
      x0 = __builtin_fmaxf(x0, __shfl_xor(x0, 32)); x1 = __builtin_fmaxf(x1, __shfl_xor(x1, 32));
      const float n0 = __builtin_fmaxf(m0, x0), n1 = __builtin_fmaxf(m1, x1);
      const float a0 = t == 0 ? 0.f : __builtin_amdgcn_exp2f((m0 - n0) * LOG2E * at), a1 = t == 0 ? 0.f : __builtin_amdgcn_exp2f((m1 - n1) * LOG2E * at);
      m0 = n0; m1 = n1;
#pragma unroll
      for (int i = 0; i < 16; ++i) {
        o0[i] *= a0; o1[i] *= a1;
        s[i] = __builtin_amdgcn_exp2f((s[i] - n0) * LOG2E * at);
        s[16 + i] = __builtin_amdgcn_exp2f((s[16 + i] - n1) * LOG2E * at);
      }
      const f32x8 p0 = __builtin_shufflevector(s, s, 0, 1, 2, 3, 4, 5, 6, 7), p1 = __builtin_shufflevector(s, s, 8, 9, 10, 11, 12, 13, 14, 15);
      const f32x8 p2 = __builtin_shufflevector(s, s, 16, 17, 18, 19, 20, 21, 22, 23), p3 = __builtin_shufflevector(s, s, 24, 25, 26, 27, 28, 29, 30, 31);
      o0 = __builtin_amdgcn_smfmac_f32_32x32x32_bf16(__builtin_bit_cast(bf16x8, v0), __builtin_bit_cast(bf16x16, p0), o0, (int)idx, 0, 0);
      o0 = __builtin_amdgcn_smfmac_f32_32x32x32_bf16(__builtin_bit_cast(bf16x8, v1), __builtin_bit_cast(bf16x16, p1), o0, (int)idx, 0, 0);
      o1 = __builtin_amdgcn_smfmac_f32_32x32x32_bf16(__builtin_bit_cast(bf16x8, v0), __builtin_bit_cast(bf16x16, p2), o1, (int)idx, 0, 0);
      o1 = __builtin_amdgcn_smfmac_f32_32x32x32_bf16(__builtin_bit_cast(bf16x8, v1), __builtin_bit_cast(bf16x16, p3), o1, (int)idx, 0, 0);
    }
    const float i0 = __builtin_amdgcn_rcpf(o0[10]), i1 = __builtin_amdgcn_rcpf(o1[10]);
    unsigned* op = O + r32 * (DH / 2) + hi * 5;
    u32x4a x;
#pragma unroll
    for (int i = 0; i < 4; ++i) x[i] = pk(o0[2 * i] * i0, o0[2 * i + 1] * i0);
    *(u32x4a*)op = x; op[4] = pk(o0[8] * i0, o0[9] * i0);
#pragma unroll
    for (int i = 0; i < 4; ++i) x[i] = pk(o1[2 * i] * i1, o1[2 * i + 1] * i1);
    *(u32x4a*)(op + 32 * (DH / 2)) = x; op[32 * (DH / 2) + 4] = pk(o1[8] * i1, o1[9] * i1);
  }
}

extern "C" void kernel_launch(void* const* d_in, const int* in_sizes, int n_in, void* d_out, int out_size, void* d_ws,
                              size_t ws_size, hipStream_t stream) {
  (void)hipFuncSetAttribute((const void*)qknorm_attn, hipFuncAttributeMaxDynamicSharedMemorySize, LDS_BYTES);
  qknorm_attn<<<256, 768, LDS_BYTES, stream>>>((const unsigned*)d_in[0], (const unsigned*)d_in[1], (const unsigned*)d_in[2],
                                               (const unsigned*)d_in[3], (unsigned*)d_out);
}
```
